```python
import torch

def _softmax(x, axis=-1):
    return torch.softmax(x, dim=axis)
def _gelu(x):
    return torch.nn.functional.gelu(x, approximate="tanh")
def _silu(x):
    return torch.nn.functional.silu(x)
def _sigmoid(x):
    return torch.sigmoid(x)
def _layernorm(x, axis=-1, eps=1e-5):
    m = x.mean(dim=axis, keepdim=True)
    v = x.var(dim=axis, keepdim=True, unbiased=False)
    return (x - m) / torch.sqrt(v + eps)
def _scatter_add(tgt, idx, src, axis=0):
    return tgt.clone().index_add_(axis, idx, src)
def _segment_sum(src, seg, n):
    out = torch.zeros((n,) + src.shape[1:], dtype=src.dtype)
    return out.index_add_(0, seg, src)
def _pad(x, pw):
    flat = tuple(v for pair in reversed(pw) for v in pair)
    return torch.nn.functional.pad(x, flat)

kernel_name = 'gated_mix__mha_block_cached__glu_gated_swiglu__decode'

def setup_inputs():

    _w = (lambda a: (lambda t: t.to(torch.bfloat16) if t.is_floating_point() else t)(torch.as_tensor(a)))
    def _A_setup():
        B, Sq, D = 1, 1, 640
        H, Sk, Dh = 32, 8192, 192
        return {
            "x": _w((lambda *s: torch.randn(*s, dtype=torch.bfloat16) * 0.05)(B, Sq, D)),
            "k_cache": _w((lambda *s: torch.randn(*s, dtype=torch.bfloat16) * 3.2)(B, H, Sk, Dh)),
            "v_cache": _w((lambda *s: torch.randn(*s, dtype=torch.bfloat16) * 0.2)(B, H, Sk, Dh)),
            "wq": _w((lambda *s: torch.randn(*s, dtype=torch.bfloat16) * 3.2)(D, H * Dh) * (D ** -0.5)),
            "wo": _w((lambda *s: torch.randn(*s, dtype=torch.bfloat16) * 0.2)(H * Dh, D) * ((H * Dh) ** -0.5)),
        }
    _A_in = _A_setup()

    def _B_setup():
        B, S, D, F = 1, 1, 640, 1536
        return {
            "x": _w((lambda *s: torch.randn(*s, dtype=torch.bfloat16) * 0.05)(B, S, D)),
            "w1": _w((lambda *s: torch.randn(*s, dtype=torch.bfloat16) * 0.4)(D, F) * (2.0 / D) ** 0.5),
            "w3": _w((lambda *s: torch.randn(*s, dtype=torch.bfloat16) * 0.4)(D, F) * (2.0 / D) ** 0.5),
            "w2": _w((lambda *s: torch.randn(*s, dtype=torch.bfloat16) * 0.4)(F, D) * (2.0 / F) ** 0.5),
        }
    _B_in = _B_setup()

    return {
        "A_x": _A_in["x"],
        "A_k_cache": _A_in["k_cache"],
        "A_v_cache": _A_in["v_cache"],
        "A_wq": _A_in["wq"],
        "A_wo": _A_in["wo"],
        "B_x": _B_in["x"],
        "B_w1": _B_in["w1"],
        "B_w3": _B_in["w3"],
        "B_w2": _B_in["w2"],
        "gate_w": _w((lambda *s: torch.randn(*s, dtype=torch.bfloat16) * 0.05)(640) * 16.0),
    }

def reference(A_x, A_k_cache, A_v_cache, A_wq, A_wo, B_x, B_w1, B_w3, B_w2, gate_w):
    def _A_ref(x, k_cache, v_cache, wq, wo):
        B, Sq, D = x.shape
        H, Dh = 32, 192
        q = (lambda x, *d: x.permute(*d))((x @ wq).reshape(B, Sq, H, Dh), 0, 2, 1, 3)
        scores = torch.einsum("bhqd,bhkd->bhqk", q, k_cache) * (Dh ** -0.5)
        p = _softmax(scores, axis=-1)
        o = torch.einsum("bhqk,bhkd->bhqd", p, v_cache)
        return (lambda x, *d: x.permute(*d))(o, 0, 2, 1, 3).reshape(B, Sq, H * Dh) @ wo

    def _B_ref(x, w1, w3, w2):
        gate = torch.einsum("bsd,df->bsf", x, w1)
        up = torch.einsum("bsd,df->bsf", x, w3)
        h = _silu(gate) * up
        out = torch.einsum("bsf,fd->bsd", h, w2)
        return out

    x = A_x
    g = _sigmoid((x * gate_w).sum(-1, keepdims=True))
    a = _A_ref(x, A_k_cache, A_v_cache, A_wq, A_wo)
    b = _B_ref(x, B_w1, B_w3, B_w2)
    return g * a + (1 - g) * b
```

```cpp
#include <hip/hip_runtime.h>
#include <hip/hip_bf16.h>
#include <stdint.h>

namespace {

typedef unsigned int u32;
typedef unsigned int u32x4 __attribute__((ext_vector_type(4)));
typedef unsigned int u32x2 __attribute__((ext_vector_type(2)));
typedef float f32x4 __attribute__((ext_vector_type(4)));
typedef __bf16 bf16x2 __attribute__((ext_vector_type(2)));

constexpr int DM = 640, NH = 32, SK = 8192, DH = 192, FF = 1536;
constexpr int NSPLIT = 8, NWG = NH * NSPLIT, NTHR = 512, NWAVE = 8;
constexpr int KEYS_WG = SK / NSPLIT;
constexpr int NTILE = KEYS_WG / (8 * NWAVE);
constexpr int ROWB = DH * 2;
#define LOOKV 11
constexpr int LOOK = LOOKV;

constexpr u32 WS_EPOCH = 0;
constexpr u32 WS_G1 = 4096;
constexpr u32 WS_GM = WS_G1 + NWG * 2048;
constexpr u32 WS_G2 = WS_GM + FF * 8;
constexpr u32 WS_END = WS_G2 + NH * DM * 8;
constexpr u32 OOB = 0x40000000u;
constexpr int AUX_NT = 2, AUX_SC1 = 16;
constexpr u32 SPIN_CAP = 40000;

constexpr int L_PART = 0;
constexpr int L_MLP = L_PART + NWAVE * DH;
constexpr int L_QB = L_MLP + NWAVE * 32;
constexpr int L_G = L_QB + 96;
constexpr int L_MW = L_G + 4;
constexpr int L_LW = L_MW + 8;
constexpr int L_GATH = L_LW + 8;
constexpr int L_GM = L_GATH + DH * 8;
constexpr int L_GL = L_GM + 8;
constexpr int L_HM = L_GL + 8;
constexpr int L_Z = L_HM + 48;
constexpr int L_END = L_Z + NWAVE * 80;

struct Params {
  const void* x; const void* k; const void* v; const void* wq; const void* wo;
  const void* w1; const void* w3; const void* w2; const void* gw; void* out; void* ws;
};

__device__ __forceinline__ float dot2(u32 a, u32 b, float c) {
  return __builtin_amdgcn_fdot2_f32_bf16(__builtin_bit_cast(bf16x2, a), __builtin_bit_cast(bf16x2, b), c, false);
}
template <int CTRL> __device__ __forceinline__ float dpp(float x) {
  return __builtin_bit_cast(float, __builtin_amdgcn_mov_dpp(__builtin_bit_cast(int, x), CTRL, 0xf, 0xf, true));
}
constexpr int XOR1 = 0xB1, XOR2 = 0x4E, XOR7 = 0x141, XOR8 = 0x128;
__device__ __forceinline__ float bflo(u32 v) { return __builtin_bit_cast(float, v << 16); }
__device__ __forceinline__ float bfhi(u32 v) { return __builtin_bit_cast(float, v & 0xffff0000u); }
__device__ __forceinline__ u32 f2u(float f) { return __builtin_bit_cast(u32, f); }
__device__ __forceinline__ float u2f(u32 u) { return __builtin_bit_cast(float, u); }
__device__ __forceinline__ u32 f2bf(float f) { return (u32)__builtin_bit_cast(unsigned short, (__bf16)f); }
__device__ __forceinline__ float rs32(float a, float b) {
  auto r = __builtin_amdgcn_permlane32_swap(f2u(a), f2u(b), false, false);
  return u2f(r[0]) + u2f(r[1]);
}
__device__ __forceinline__ float rs16(float a, float b) {
  auto r = __builtin_amdgcn_permlane16_swap(f2u(a), f2u(b), false, false);
  return u2f(r[0]) + u2f(r[1]);
}
__device__ __forceinline__ float max32(float a) {
  auto r = __builtin_amdgcn_permlane32_swap(f2u(a), f2u(a), false, false);
  return fmaxf(u2f(r[0]), u2f(r[1]));
}
__device__ __forceinline__ float max16(float a) {
  auto r = __builtin_amdgcn_permlane16_swap(f2u(a), f2u(a), false, false);
  return fmaxf(u2f(r[0]), u2f(r[1]));
}
__device__ __forceinline__ __amdgpu_buffer_rsrc_t mk_rsrc(const void* base, u32 bytes) {
  return __builtin_amdgcn_make_buffer_rsrc(const_cast<void*>(base), 0, bytes, 0x00020000);
}
#define SB() __builtin_amdgcn_sched_barrier(0)

__device__ __forceinline__ void reduce24_store(const float (&a)[24], float* dst, int lane) {
  float b[12], c[6], d[3];
#pragma unroll
  for (int i = 0; i < 12; ++i) b[i] = rs32(a[i], a[i + 12]);
#pragma unroll
  for (int i = 0; i < 6; ++i) c[i] = rs16(b[i], b[i + 6]);
  const bool hi8 = (lane & 8) != 0;
#pragma unroll
  for (int i = 0; i < 3; ++i) {
    const float t0 = c[i] + dpp<XOR8>(c[i]), t1 = c[i + 3] + dpp<XOR8>(c[i + 3]);
    d[i] = hi8 ? t1 : t0;
  }
  const int j = lane & 7;
  const int i0 = 12 * ((lane >> 5) & 1) + 6 * ((lane >> 4) & 1) + 3 * ((lane >> 3) & 1);
#pragma unroll
  for (int i = 0; i < 3; ++i) {
    const int idx = i0 + i;
    dst[((idx >> 3) * 8 + j) * 8 + (idx & 7)] = d[i];
  }
}

__global__ void __launch_bounds__(NTHR, 2) fused_decode_kernel(Params p) {
  __shared__ __attribute__((aligned(16))) float lds[L_END];
  const int tid = threadIdx.x, lane = tid & 63;
  const int w = __builtin_amdgcn_readfirstlane(tid >> 6);
  const int b = blockIdx.x;
  const int h = 4 * (b & 7) + (b >> 6), s = (b >> 3) & 7;
  const int r = lane >> 3, j = lane & 7;
  constexpr float C2 = 0.07216878364870322f * 1.4426950408889634f;

  const __amdgpu_buffer_rsrc_t rWS = mk_rsrc(p.ws, WS_END);
  const __amdgpu_buffer_rsrc_t rX = mk_rsrc(p.x, DM * 2);
  const __amdgpu_buffer_rsrc_t rGW = mk_rsrc(p.gw, DM * 2);
  const __amdgpu_buffer_rsrc_t rWQ = mk_rsrc((const char*)p.wq + h * ROWB, DM * NH * ROWB - h * ROWB);
  const u32 fcol = (48 * h + 8 * s) * 2;
  const u32 mlp_bytes = s < 6 ? (u32)(DM * FF * 2) - fcol : 0u;
  const __amdgpu_buffer_rsrc_t rW1 = mk_rsrc((const char*)p.w1 + (s < 6 ? fcol : 0u), mlp_bytes);
  const __amdgpu_buffer_rsrc_t rW3 = mk_rsrc((const char*)p.w3 + (s < 6 ? fcol : 0u), mlp_bytes);
  const size_t kvoff = ((size_t)h * SK + (size_t)s * KEYS_WG) * ROWB;
  const __amdgpu_buffer_rsrc_t rK = mk_rsrc((const char*)p.k + kvoff, KEYS_WG * ROWB);
  const __amdgpu_buffer_rsrc_t rV = mk_rsrc((const char*)p.v + kvoff, KEYS_WG * ROWB);
  const __amdgpu_buffer_rsrc_t rWO = mk_rsrc((const char*)p.wo + (size_t)h * DH * DM * 2 + s * 160, DH * DM * 2 - s * 160);
  const __amdgpu_buffer_rsrc_t rW2 = mk_rsrc((const char*)p.w2 + (size_t)h * 48 * DM * 2 + s * 160, 48 * DM * 2 - s * 160);

  const u32 epoch = __builtin_amdgcn_raw_buffer_load_b32(rWS, WS_EPOCH, 0, AUX_SC1);
  u32 xp[5];
#pragma unroll
  for (int u = 0; u < 5; ++u) xp[u] = __builtin_amdgcn_raw_buffer_load_b32(rX, (w * 40 + u * 8 + r) * 4, 0, 0);
  const u32 moff = lane < 40 ? (u32)(w * 40 + lane) * 4u : OOB;
  const u32 xm = __builtin_amdgcn_raw_buffer_load_b32(rX, moff, 0, 0);
  const u32 gm = __builtin_amdgcn_raw_buffer_load_b32(rGW, moff, 0, 0);
  SB();
  u32x4 wqv[5][2][3];
  const u32 wq_voff = (u32)(w * 80 + 2 * r) * (NH * ROWB) + j * 16;
#pragma unroll
  for (int u = 0; u < 5; ++u)
#pragma unroll
    for (int a = 0; a < 2; ++a)
#pragma unroll
      for (int c = 0; c < 3; ++c)
        wqv[u][a][c] = __builtin_amdgcn_raw_buffer_load_b128(rWQ, wq_voff + c * 128, (u * 16 + a) * (NH * ROWB), 0);
  SB();
  const u32 m_voff = lane < 40 ? (u32)(w * 80 + 2 * lane) * (FF * 2) : OOB;
  const u32x4 w1a = __builtin_amdgcn_raw_buffer_load_b128(rW1, m_voff, 0, 0);
  const u32x4 w1b = __builtin_amdgcn_raw_buffer_load_b128(rW1, m_voff + FF * 2, 0, 0);
  const u32x4 w3a = __builtin_amdgcn_raw_buffer_load_b128(rW3, m_voff, 0, 0);
  const u32x4 w3b = __builtin_amdgcn_raw_buffer_load_b128(rW3, m_voff + FF * 2, 0, 0);
  SB();
  u32x4 kv[2 * NTILE][3];
  const u32 kv_voff = (u32)(w * 8 + r) * ROWB + j * 16;
#define ISSUE_KV(i_)                                                                                              \
  do {                                                                                                            \
    _Pragma("unroll") for (int c_ = 0; c_ < 3; ++c_)                                                              \
        kv[(i_)][c_] = __builtin_amdgcn_raw_buffer_load_b128((i_) < NTILE ? rK : rV, kv_voff + c_ * 128,          \
                                                            ((i_) & (NTILE - 1)) * (8 * NWAVE * ROWB), AUX_NT);   \
  } while (0)
  constexpr int K0 = LOOK - 10;
#pragma unroll
  for (int i = 0; i < K0; ++i) ISSUE_KV(i);
  SB();
  {
    const u32x4 z4 = {0u, 0u, 0u, 0u};
    const u32 g1o = tid < 97 ? WS_G1 + (u32)(h * 8 + s) * 2048u + tid * 16u : OOB;
    __builtin_amdgcn_raw_buffer_store_b128(z4, rWS, g1o, 0, AUX_SC1);
    const u32 g2o = tid < 40 ? WS_G2 + (u32)(h * DM + s * 80) * 8u + tid * 16u : OOB;
    __builtin_amdgcn_raw_buffer_store_b128(z4, rWS, g2o, 0, AUX_SC1);
    const u32x2 z2 = {0u, 0u};
    const u32 gmo = (tid >= 192 && tid < 200 && s < 6) ? WS_GM + (u32)(48 * h + 8 * s + (tid - 192)) * 8u : OOB;
    __builtin_amdgcn_raw_buffer_store_b64(z2, rWS, gmo, 0, AUX_SC1);
  }
  SB();

  float acc[24];
#pragma unroll
  for (int i = 0; i < 24; ++i) acc[i] = 0.f;
#pragma unroll
  for (int u = 0; u < 5; ++u) {
#pragma unroll
    for (int c = 0; c < 3; ++c)
#pragma unroll
      for (int k = 0; k < 4; ++k) {
        const u32 ra = wqv[u][0][c][k], rb = wqv[u][1][c][k];
        acc[8 * c + 2 * k] = dot2(__builtin_amdgcn_perm(rb, ra, 0x05040100u), xp[u], acc[8 * c + 2 * k]);
        acc[8 * c + 2 * k + 1] = dot2(__builtin_amdgcn_perm(rb, ra, 0x07060302u), xp[u], acc[8 * c + 2 * k + 1]);
      }
    SB();
    ISSUE_KV(K0 + 2 * u);
    ISSUE_KV(K0 + 1 + 2 * u);
    SB();
  }
  reduce24_store(acc, lds + L_PART + w * DH, lane);
  {
    float m[16];
#pragma unroll
    for (int k = 0; k < 4; ++k) {
      m[2 * k] = dot2(__builtin_amdgcn_perm(w1b[k], w1a[k], 0x05040100u), xm, 0.f);
      m[2 * k + 1] = dot2(__builtin_amdgcn_perm(w1b[k], w1a[k], 0x07060302u), xm, 0.f);
      m[8 + 2 * k] = dot2(__builtin_amdgcn_perm(w3b[k], w3a[k], 0x05040100u), xm, 0.f);
      m[8 + 2 * k + 1] = dot2(__builtin_amdgcn_perm(w3b[k], w3a[k], 0x07060302u), xm, 0.f);
    }
    float gs = dot2(xm, gm, 0.f);
    float m8[8], m4[4], m2[2];
#pragma unroll
    for (int i = 0; i < 8; ++i) m8[i] = rs32(m[i], m[i + 8]);
#pragma unroll
    for (int i = 0; i < 4; ++i) m4[i] = rs16(m8[i], m8[i + 4]);
#pragma unroll
    for (int i = 0; i < 2; ++i) {
      const float t0 = m4[i] + dpp<XOR8>(m4[i]), t1 = m4[i + 2] + dpp<XOR8>(m4[i + 2]);
      m2[i] = (lane & 8) ? t1 : t0;
    }
    const float t0 = m2[0] + dpp<XOR7>(m2[0]), t1 = m2[1] + dpp<XOR7>(m2[1]);
    float m1 = (lane & 4) ? t1 : t0;
    m1 += dpp<XOR2>(m1);
    m1 += dpp<XOR1>(m1);
    if ((lane & 3) == 0) lds[L_MLP + w * 32 + (lane >> 2)] = m1;
    gs += dpp<XOR1>(gs); gs += dpp<XOR2>(gs); gs += dpp<XOR7>(gs); gs += dpp<XOR8>(gs);
    gs = rs16(gs, gs); gs = rs32(gs, gs);
    if (lane == 0) lds[L_MLP + w * 32 + 16] = gs;
  }
  __syncthreads();
  const u32 tag = (epoch + 1u == 0u) ? 1u : epoch + 1u;
  if (tid < 96) {
    float q0 = 0.f, q1 = 0.f;
#pragma unroll
    for (int ww = 0; ww < NWAVE; ++ww) { q0 += lds[L_PART + ww * DH + 2 * tid]; q1 += lds[L_PART + ww * DH + 2 * tid + 1]; }
    lds[L_QB + tid] = u2f(f2bf(q0) | (f2bf(q1) << 16));
  } else if (tid >= 192 && tid < 200) {
    float ga = 0.f, up = 0.f;
#pragma unroll
    for (int ww = 0; ww < NWAVE; ++ww) { ga += lds[L_MLP + ww * 32 + (tid - 192)]; up += lds[L_MLP + ww * 32 + 8 + (tid - 192)]; }
    const float hv = ga / (1.f + __expf(-ga)) * up;
    const u32x2 gr = {f2u(hv), tag};
    const u32 gmo = s < 6 ? WS_GM + (u32)(48 * h + 8 * s + (tid - 192)) * 8u : OOB;
    __builtin_amdgcn_raw_buffer_store_b64(gr, rWS, gmo, 0, AUX_SC1);
  } else if (tid == 256) {
    float gs = 0.f;
#pragma unroll
    for (int ww = 0; ww < NWAVE; ++ww) gs += lds[L_MLP + ww * 32 + 16];
    lds[L_G] = 1.f / (1.f + __expf(-gs));
  }
  __syncthreads();
  u32x4 qv[3];
#pragma unroll
  for (int c = 0; c < 3; ++c) qv[c] = *reinterpret_cast<const u32x4*>(lds + L_QB + (8 * c + j) * 4);
  SB();

  float sc[NTILE];
#pragma unroll
  for (int t = 0; t < NTILE; ++t) {
    float d0 = 0.f, d1 = 0.f, d2 = 0.f;
#pragma unroll
    for (int k = 0; k < 4; ++k) {
      d0 = dot2(kv[t][0][k], qv[0][k], d0);
      d1 = dot2(kv[t][1][k], qv[1][k], d1);
      d2 = dot2(kv[t][2][k], qv[2][k], d2);
    }
    SB();
    ISSUE_KV(t + LOOK);
    SB();
    float d = (d0 + d1) + d2;
    d += dpp<XOR1>(d); d += dpp<XOR2>(d); d += dpp<XOR7>(d);
    sc[t] = d;
  }
  float mw = sc[0];
#pragma unroll
  for (int t = 1; t < NTILE; ++t) mw = fmaxf(mw, sc[t]);
  mw = fmaxf(mw, dpp<XOR8>(mw)); mw = max16(mw); mw = max32(mw);
  float lsum = 0.f;
  const float mc = mw * C2;
#pragma unroll
  for (int t = 0; t < NTILE; ++t) { sc[t] = __builtin_amdgcn_exp2f(fmaf(sc[t], C2, -mc)); lsum += sc[t]; }
  lsum += dpp<XOR8>(lsum); lsum = rs16(lsum, lsum); lsum = rs32(lsum, lsum);
  if (lane == 0) { lds[L_MW + w] = mw; lds[L_LW + w] = lsum; }
  SB();

  u32x4 wov[8];
  const int g4 = lane >> 4, c16 = lane & 15;
#pragma unroll
  for (int i = 0; i < 24; ++i) acc[i] = 0.f;
#pragma unroll
  for (int t = 0; t < NTILE; ++t) {
    const float pt = sc[t];
#pragma unroll
    for (int c = 0; c < 3; ++c)
#pragma unroll
      for (int k = 0; k < 4; ++k) {
        const u32 vv = kv[NTILE + t][c][k];
        acc[8 * c + 2 * k] = fmaf(pt, bflo(vv), acc[8 * c + 2 * k]);
        acc[8 * c + 2 * k + 1] = fmaf(pt, bfhi(vv), acc[8 * c + 2 * k + 1]);
      }
    SB();
    if (NTILE + t + LOOK < 2 * NTILE) {
      ISSUE_KV(NTILE + t + LOOK < 2 * NTILE ? NTILE + t + LOOK : 0);
    } else if (NTILE + t + LOOK == 2 * NTILE) {
      const u32 wo_voff = c16 < 10 ? (u32)(w * 24 + g4) * (DM * 2) + c16 * 16 : OOB;
      const u32 w2_voff = c16 < 10 ? (u32)(w * 6 + g4) * (DM * 2) + c16 * 16 : OOB;
      const u32 w2_voff2 = (c16 < 10 && g4 < 2) ? (u32)(w * 6 + 4 + g4) * (DM * 2) + c16 * 16 : OOB;
      wov[6] = __builtin_amdgcn_raw_buffer_load_b128(rW2, w2_voff, 0, 0);
      wov[7] = __builtin_amdgcn_raw_buffer_load_b128(rW2, w2_voff2, 0, 0);
#pragma unroll
      for (int i = 0; i < 6; ++i) wov[i] = __builtin_amdgcn_raw_buffer_load_b128(rWO, wo_voff, i * 4 * (DM * 2), 0);
    }
    SB();
  }
  reduce24_store(acc, lds + L_PART + w * DH, lane);
  __syncthreads();

  if (tid < 128) {
    float M = lds[L_MW];
#pragma unroll
    for (int ww = 1; ww < NWAVE; ++ww) M = fmaxf(M, lds[L_MW + ww]);
    float o0 = 0.f, o1 = 0.f, L = 0.f;
    const int d = tid < 96 ? 2 * tid : 0;
#pragma unroll
    for (int ww = 0; ww < NWAVE; ++ww) {
      const float e = __builtin_amdgcn_exp2f((lds[L_MW + ww] - M) * C2);
      o0 = fmaf(e, lds[L_PART + ww * DH + d], o0);
      o1 = fmaf(e, lds[L_PART + ww * DH + d + 1], o1);
      L = fmaf(e, lds[L_LW + ww], L);
    }
    const u32x4 pc = {f2u(tid < 96 ? o0 : M), tag, f2u(tid < 96 ? o1 : L), tag};
    const u32 g1o = tid < 97 ? WS_G1 + (u32)(h * 8 + s) * 2048u + tid * 16u : OOB;
    __builtin_amdgcn_raw_buffer_store_b128(pc, rWS, g1o, 0, AUX_SC1);
  }
  {
    const int e = tid & 127, s0 = tid >> 7;
    const bool a0 = e < 97, hm0 = (s0 == 0) && e >= 97 && e < 121;
    const u32 off0 = a0 ? WS_G1 + (u32)(h * 8 + s0) * 2048u + e * 16u
                        : (hm0 ? WS_GM + (u32)(48 * h + 2 * (e - 97)) * 8u : OOB);
    const u32 off1 = a0 ? WS_G1 + (u32)(h * 8 + s0 + 4) * 2048u + e * 16u : OOB;
    const bool act0 = a0 || hm0;
    u32x4 va, vb;
    for (u32 spin = 0;; ++spin) {
      asm volatile("" ::: "memory");
      va = __builtin_amdgcn_raw_buffer_load_b128(rWS, off0, 0, AUX_SC1);
      vb = __builtin_amdgcn_raw_buffer_load_b128(rWS, off1, 0, AUX_SC1);
      const bool ok = (!act0 || (va[1] == tag && va[3] == tag)) && (!a0 || (vb[1] == tag && vb[3] == tag));
      if (__all(ok) || spin > SPIN_CAP) break;
      __builtin_amdgcn_s_sleep(1);
    }
    if (e < 96) {
      lds[L_GATH + (2 * e) * 8 + s0] = u2f(va[0]);
      lds[L_GATH + (2 * e + 1) * 8 + s0] = u2f(va[2]);
      lds[L_GATH + (2 * e) * 8 + s0 + 4] = u2f(vb[0]);
      lds[L_GATH + (2 * e + 1) * 8 + s0 + 4] = u2f(vb[2]);
    } else if (e == 96) {
      lds[L_GM + s0] = u2f(va[0]); lds[L_GL + s0] = u2f(va[2]);
      lds[L_GM + s0 + 4] = u2f(vb[0]); lds[L_GL + s0 + 4] = u2f(vb[2]);
    } else if (hm0) {
      lds[L_HM + 2 * (e - 97)] = u2f(va[0]);
      lds[L_HM + 2 * (e - 97) + 1] = u2f(va[2]);
    }
  }
  __syncthreads();

  {
    const f32x4 m0 = *reinterpret_cast<const f32x4*>(lds + L_GM), m1 = *reinterpret_cast<const f32x4*>(lds + L_GM + 4);
    const f32x4 l0 = *reinterpret_cast<const f32x4*>(lds + L_GL), l1 = *reinterpret_cast<const f32x4*>(lds + L_GL + 4);
    const float M = fmaxf(fmaxf(fmaxf(m0[0], m0[1]), fmaxf(m0[2], m0[3])), fmaxf(fmaxf(m1[0], m1[1]), fmaxf(m1[2], m1[3])));
    float ws[8], L = 0.f;
#pragma unroll
    for (int i = 0; i < 4; ++i) {
      ws[i] = __builtin_amdgcn_exp2f((m0[i] - M) * C2);
      ws[4 + i] = __builtin_amdgcn_exp2f((m1[i] - M) * C2);
    }
#pragma unroll
    for (int i = 0; i < 4; ++i) L = fmaf(ws[i], l0[i], L);
#pragma unroll
    for (int i = 0; i < 4; ++i) L = fmaf(ws[4 + i], l1[i], L);
    const float g = lds[L_G];
    const float sca = g / L, scb = 1.f - g;
    float z[8];
#pragma unroll
    for (int i = 0; i < 8; ++i) z[i] = 0.f;
#pragma unroll
    for (int i = 0; i < 8; ++i) {
      float coef;
      if (i < 6) {
        const int d = w * 24 + 4 * i + g4;
        const f32x4 a0 = *reinterpret_cast<const f32x4*>(lds + L_GATH + d * 8);
        const f32x4 a1 = *reinterpret_cast<const f32x4*>(lds + L_GATH + d * 8 + 4);
        float o = 0.f;
#pragma unroll
        for (int q = 0; q < 4; ++q) o = fmaf(ws[q], a0[q], o);
#pragma unroll
        for (int q = 0; q < 4; ++q) o = fmaf(ws[4 + q], a1[q], o);
        coef = o * sca;
      } else if (i == 6) {
        coef = lds[L_HM + w * 6 + g4] * scb;
      } else {
        coef = lds[L_HM + w * 6 + 4 + (g4 & 1)] * scb;
      }
#pragma unroll
      for (int k = 0; k < 4; ++k) {
        z[2 * k] = fmaf(coef, bflo(wov[i][k]), z[2 * k]);
        z[2 * k + 1] = fmaf(coef, bfhi(wov[i][k]), z[2 * k + 1]);
      }
    }
    float z4[4], z2[2];
#pragma unroll
    for (int i = 0; i < 4; ++i) z4[i] = rs32(z[i], z[i + 4]);
#pragma unroll
    for (int i = 0; i < 2; ++i) z2[i] = rs16(z4[i], z4[i + 2]);
    if (c16 < 10) {
      float* dst = lds + L_Z + w * 80 + c16 * 8 + 4 * (lane >> 5) + 2 * ((lane >> 4) & 1);
      dst[0] = z2[0]; dst[1] = z2[1];
    }
  }
  __syncthreads();
  if (w != 0) return;
  {
    float z0 = 0.f, z1 = 0.f;
    const int c = lane < 40 ? 2 * lane : 0;
#pragma unroll
    for (int ww = 0; ww < NWAVE; ++ww) { z0 += lds[L_Z + ww * 80 + c]; z1 += lds[L_Z + ww * 80 + c + 1]; }
    const u32x4 pc = {f2u(z0), tag, f2u(z1), tag};
    const u32 g2o = lane < 40 ? WS_G2 + (u32)(h * DM + s * 80) * 8u + lane * 16u : OOB;
    __builtin_amdgcn_raw_buffer_store_b128(pc, rWS, g2o, 0, AUX_SC1);
  }
  if (h >= 10) return;
  {
    const int ph = lane & 31, half = lane >> 5;
    const int c0 = s * 80 + h * 8 + half * 4;
    const u32 off = WS_G2 + (u32)(ph * DM + c0) * 8u;
    u32x4 va, vb;
    for (u32 spin = 0;; ++spin) {
      asm volatile("" ::: "memory");
      va = __builtin_amdgcn_raw_buffer_load_b128(rWS, off, 0, AUX_SC1);
      vb = __builtin_amdgcn_raw_buffer_load_b128(rWS, off + 16, 0, AUX_SC1);
      const bool ok = va[1] == tag && va[3] == tag && vb[1] == tag && vb[3] == tag;
      if (__all(ok) || spin > SPIN_CAP) break;
      __builtin_amdgcn_s_sleep(1);
    }
    float o[4] = {u2f(va[0]), u2f(va[2]), u2f(vb[0]), u2f(vb[2])};
#pragma unroll
    for (int i = 0; i < 4; ++i) {
      float t = o[i];
      t += dpp<XOR1>(t); t += dpp<XOR2>(t); t += dpp<XOR7>(t); t += dpp<XOR8>(t);
      o[i] = rs16(t, t);
    }
    if (ph == 0) {
      const u32x2 ov = {f2bf(o[0]) | (f2bf(o[1]) << 16), f2bf(o[2]) | (f2bf(o[3]) << 16)};
      *reinterpret_cast<u32x2*>(reinterpret_cast<char*>(p.out) + c0 * 2) = ov;
    }
    if (b == 0 && lane == 0) __builtin_amdgcn_raw_buffer_store_b32(tag, rWS, WS_EPOCH, 0, AUX_SC1);
  }
}

}

extern "C" void kernel_launch(void* const* d_in, const int* in_sizes, int n_in, void* d_out, int out_size, void* d_ws,
                              size_t ws_size, hipStream_t stream) {
  (void)in_sizes; (void)n_in; (void)out_size; (void)ws_size;
  Params p;
  p.x = d_in[0]; p.k = d_in[1]; p.v = d_in[2]; p.wq = d_in[3]; p.wo = d_in[4];
  p.w1 = d_in[6]; p.w3 = d_in[7]; p.w2 = d_in[8]; p.gw = d_in[9];
  p.out = d_out; p.ws = d_ws;
  hipLaunchKernelGGL(fused_decode_kernel, dim3(NWG), dim3(NTHR), 0, stream, p);
}
```

```cpp
#include <hip/hip_runtime.h>
#include <hip/hip_bf16.h>
#include <stdint.h>
#include <type_traits>

namespace {

typedef unsigned int u32;
typedef unsigned int u32x4 __attribute__((ext_vector_type(4)));
typedef unsigned int u32x2 __attribute__((ext_vector_type(2)));
typedef float f32x4 __attribute__((ext_vector_type(4)));
typedef __bf16 bf16x2 __attribute__((ext_vector_type(2)));

constexpr int DM = 640, NH = 32, SK = 8192, DH = 192, FF = 1536;
constexpr int NSPLIT = 8, NWG = NH * NSPLIT, NTHR = 512, NWAVE = 8;
constexpr int ROWB = DH * 2;
constexpr int TILEB = 8 * NWAVE * ROWB;
constexpr int NS = SK / NSPLIT / (8 * NWAVE);
constexpr int NMLP = FF / 64;
constexpr int K0 = 2;
constexpr int LOOK = 10;
constexpr int LAG = 2;
constexpr int cons_end(int i) {
  return i < LOOK ? 0 : (2 * (i - LOOK + 1) < i - LAG + 1 ? 2 * (i - LOOK + 1) : (i - LAG + 1 < 2 * NS ? i - LAG + 1 : 2 * NS));
}
constexpr int W2_LD = NS + 4, W2_USE = NS + 8;
constexpr int ML_LD = 2 * NS - 6, ML_USE = 2 * NS - 2;
constexpr int PACE = 93, PACE0 = 50, WDLY = 25;

constexpr u32 WS_EPOCH = 0;
constexpr u32 WS_GML = 4096;
constexpr u32 WS_GQ = 8192;
constexpr u32 WS_G1 = WS_GQ + NWG * 1280;
constexpr u32 WS_G2 = WS_G1 + NWG * 1024;
constexpr u32 WS_END = WS_G2 + 80 * 2048;
constexpr u32 OOB = 0x40000000u;
constexpr int AUX_NT = 2, AUX_SC1 = 16;
constexpr u32 SPIN_CAP = 40000;

constexpr int L_PQ = 0;
constexpr int L_QB = L_PQ + NWAVE * 320;
constexpr int L_G = L_QB + 96;
constexpr int L_HM = L_G + 4;
constexpr int L_MW = L_HM + 32;
constexpr int L_LW = L_MW + 8;
constexpr int L_PART = L_LW + 8;
constexpr int L_Z = L_PART + NWAVE * DH;
constexpr int L_CTL = L_Z + 4 * 192;
constexpr int C_PCNT = 0, C_KCNT = 1, C_ZCNT = 2;
constexpr int L_END = L_CTL + 8;
static_assert(LAG >= 2 && LOOK <= NS && cons_end(NS + LAG - 1) == NS, "all scores are known when the first V tile is consumed");

struct Params {
  const void* x; const void* k; const void* v; const void* wq; const void* wo;
  const void* w1; const void* w3; const void* w2; const void* gw; void* out; void* ws;
};

__device__ __forceinline__ float dot2(u32 a, u32 b, float c) {
  return __builtin_amdgcn_fdot2_f32_bf16(__builtin_bit_cast(bf16x2, a), __builtin_bit_cast(bf16x2, b), c, false);
}
template <int CTRL> __device__ __forceinline__ float dpp(float x) {
  return __builtin_bit_cast(float, __builtin_amdgcn_mov_dpp(__builtin_bit_cast(int, x), CTRL, 0xf, 0xf, true));
}
constexpr int XOR1 = 0xB1, XOR2 = 0x4E, XOR7 = 0x141, XOR8 = 0x128;
__device__ __forceinline__ float bflo(u32 v) { return __builtin_bit_cast(float, v << 16); }
__device__ __forceinline__ float bfhi(u32 v) { return __builtin_bit_cast(float, v & 0xffff0000u); }
__device__ __forceinline__ u32 f2u(float f) { return __builtin_bit_cast(u32, f); }
__device__ __forceinline__ float u2f(u32 u) { return __builtin_bit_cast(float, u); }
__device__ __forceinline__ u32 f2bf(float f) { return (u32)__builtin_bit_cast(unsigned short, (__bf16)f); }
__device__ __forceinline__ float rs32(float a, float b) {
  auto r = __builtin_amdgcn_permlane32_swap(f2u(a), f2u(b), false, false);
  return u2f(r[0]) + u2f(r[1]);
}
__device__ __forceinline__ float rs16(float a, float b) {
  auto r = __builtin_amdgcn_permlane16_swap(f2u(a), f2u(b), false, false);
  return u2f(r[0]) + u2f(r[1]);
}
__device__ __forceinline__ float max32(float a) {
  auto r = __builtin_amdgcn_permlane32_swap(f2u(a), f2u(a), false, false);
  return fmaxf(u2f(r[0]), u2f(r[1]));
}
__device__ __forceinline__ float max16(float a) {
  auto r = __builtin_amdgcn_permlane16_swap(f2u(a), f2u(a), false, false);
  return fmaxf(u2f(r[0]), u2f(r[1]));
}
__device__ __forceinline__ float sum64(float t) {
  t += dpp<XOR1>(t); t += dpp<XOR2>(t); t += dpp<XOR7>(t); t += dpp<XOR8>(t);
  t = rs16(t, t);
  return rs32(t, t);
}
__device__ __forceinline__ __amdgpu_buffer_rsrc_t mk_rsrc(const void* base, u32 bytes) {
  return __builtin_amdgcn_make_buffer_rsrc(const_cast<void*>(base), 0, bytes, 0x00020000);
}
#define SB() __builtin_amdgcn_sched_barrier(0)
#define PIN24(a_) asm volatile("" : "+v"(a_[0]), "+v"(a_[1]), "+v"(a_[2]), "+v"(a_[3]), "+v"(a_[4]), "+v"(a_[5]), "+v"(a_[6]), "+v"(a_[7]), \
    "+v"(a_[8]), "+v"(a_[9]), "+v"(a_[10]), "+v"(a_[11]), "+v"(a_[12]), "+v"(a_[13]), "+v"(a_[14]), "+v"(a_[15]), \
    "+v"(a_[16]), "+v"(a_[17]), "+v"(a_[18]), "+v"(a_[19]), "+v"(a_[20]), "+v"(a_[21]), "+v"(a_[22]), "+v"(a_[23]))
#define LD128(rs_, vo_, so_, aux_) __builtin_amdgcn_raw_buffer_load_b128((rs_), (vo_), (so_), (aux_))
#define ST128(v_, rs_, vo_, aux_) __builtin_amdgcn_raw_buffer_store_b128((v_), (rs_), (vo_), 0, (aux_))

template <int N> __device__ __forceinline__ void reduce_store(const float (&a)[N], float* dst, int lane, float scale) {
  float b[N / 2], c[N / 4], d[N / 8];
#pragma unroll
  for (int i = 0; i < N / 2; ++i) b[i] = rs32(a[i], a[i + N / 2]);
#pragma unroll
  for (int i = 0; i < N / 4; ++i) c[i] = rs16(b[i], b[i + N / 4]);
  const bool hi8 = (lane & 8) != 0;
#pragma unroll
  for (int i = 0; i < N / 8; ++i) {
    const float t0 = c[i] + dpp<XOR8>(c[i]), t1 = c[i + N / 8] + dpp<XOR8>(c[i + N / 8]);
    d[i] = hi8 ? t1 : t0;
  }
  const int j = lane & 7;
  const int i0 = (N / 2) * ((lane >> 5) & 1) + (N / 4) * ((lane >> 4) & 1) + (N / 8) * ((lane >> 3) & 1);
#pragma unroll
  for (int i = 0; i < N / 8; ++i) {
    const int idx = i0 + i;
    dst[(idx >> 3) * 64 + j * 8 + (idx & 7)] = d[i] * scale;
  }
}

template <int I, int N, class F> __device__ __forceinline__ void static_for(F&& f) {
  if constexpr (I < N) { f(std::integral_constant<int, I>{}); static_for<I + 1, N>(f); }
}
constexpr u32 TAGM = 0xfffu;
__device__ __forceinline__ u32 packt(float v, u32 tg) { return ((f2u(v) + 0x800u) & ~TAGM) | tg; }
__device__ __forceinline__ float unpackt(u32 v) { return u2f(v & ~TAGM); }
__device__ __forceinline__ float unsplit(u32 hi, u32 lo) { return u2f((hi & ~TAGM) | ((lo >> 12) & TAGM)); }
__device__ __forceinline__ bool hast(const u32x4& v, u32 tg) {
  return (((v[0] ^ tg) | (v[1] ^ tg) | (v[2] ^ tg) | (v[3] ^ tg)) & TAGM) == 0u;
}
typedef __attribute__((address_space(3))) u32 lds_u32;
__device__ __forceinline__ void lds_st(lds_u32* p, u32 v) { __hip_atomic_store(p, v, __ATOMIC_RELAXED, __HIP_MEMORY_SCOPE_WORKGROUP); }
#define LDS_FENCE() asm volatile("s_waitcnt lgkmcnt(0)" ::: "memory")

__global__ void __launch_bounds__(NTHR, 2) fused_decode_kernel(Params p) {
  __shared__ __attribute__((aligned(16))) float lds[L_END];
  lds_u32* ctl = (lds_u32*)(lds + L_CTL);
  const int tid = threadIdx.x, lane = tid & 63;
  const int w = __builtin_amdgcn_readfirstlane(tid >> 6);
  const int b = blockIdx.x;
  const int h = b >> 3, s = b & 7;
  const int rh = s & 1, cq = s >> 1;
  const int c0 = 320 * (cq >> 1) + 192 * (cq & 1), nch = (cq & 1) ? 16 : 24;
  const int r = lane >> 3, j = lane & 7;
  constexpr float C2 = 0.07216878364870322f * 1.4426950408889634f;
  const u32 t_pace = (u32)__builtin_amdgcn_s_memrealtime() + (u32)PACE0 + (u32)(w * PACE / 8);

  const __amdgpu_buffer_rsrc_t rWS = mk_rsrc(p.ws, WS_END);
  const __amdgpu_buffer_rsrc_t rX = mk_rsrc(p.x, DM * 2);
  const __amdgpu_buffer_rsrc_t rWQ = mk_rsrc((const char*)p.wq + h * ROWB, DM * NH * ROWB - h * ROWB);
  const u32 mlp_bytes = h < NMLP ? (u32)(DM * FF * 2 - h * 128) : 0u;
  const __amdgpu_buffer_rsrc_t rW1 = mk_rsrc((const char*)p.w1 + (h < NMLP ? h * 128 : 0), mlp_bytes);
  const __amdgpu_buffer_rsrc_t rW3 = mk_rsrc((const char*)p.w3 + (h < NMLP ? h * 128 : 0), mlp_bytes);
  const int row0 = 80 * s + 10 * w + 2 * r;
  const u32 wq_voff = r < 5 ? (u32)row0 * (NH * ROWB) + j * 16 : OOB;
  const u32 wm_voff = r < 5 ? (u32)row0 * (FF * 2) + j * 16 : OOB;
  u32x4 pa[5], pb[5];
#pragma unroll
  for (int c = 0; c < 3; ++c) {
    pa[c] = LD128(rWQ, wq_voff + c * 128, 0, AUX_NT);
    pb[c] = LD128(rWQ, wq_voff + c * 128, NH * ROWB, AUX_NT);
  }
  pa[3] = LD128(rW1, wm_voff, 0, AUX_NT);
  pb[3] = LD128(rW1, wm_voff + FF * 2, 0, AUX_NT);
  pa[4] = LD128(rW3, wm_voff, 0, AUX_NT);
  pb[4] = LD128(rW3, wm_voff + FF * 2, 0, AUX_NT);
  const u32 xp = __builtin_amdgcn_raw_buffer_load_b32(rX, r < 5 ? (u32)row0 * 2u : OOB, 0, 0);
  const u32 epoch = __builtin_amdgcn_raw_buffer_load_b32(rWS, WS_EPOCH, 0, AUX_SC1);
  const __amdgpu_buffer_rsrc_t rGW = mk_rsrc(p.gw, DM * 2);
  const u32 g_voff = (w == 3 && lane < 40) ? lane * 32u : OOB;
  const u32x4 gx0 = LD128(rX, g_voff, 0, 0), gx1 = LD128(rX, g_voff + 16, 0, 0);
  const u32x4 gw0 = LD128(rGW, g_voff, 0, 0), gw1 = LD128(rGW, g_voff + 16, 0, 0);
  SB();
  __builtin_amdgcn_s_barrier();
  SB();
  const size_t kv0 = ((size_t)h * SK + (size_t)s * (SK / NSPLIT)) * ROWB;
  const __amdgpu_buffer_rsrc_t rK = mk_rsrc((const char*)p.k + kv0, NS * TILEB);
  const __amdgpu_buffer_rsrc_t rV = mk_rsrc((const char*)p.v + kv0, NS * TILEB);
  u32x4 kv[2 * NS][3];
  const u32 kv_voff = (u32)(w * 8 + r) * ROWB + j * 16;
#define ISSUE_KV(i_)                                                                                              \
  do {                                                                                                            \
    if ((i_) >= K0)                                                                                               \
      while ((int)((u32)__builtin_amdgcn_s_memrealtime() - t_pace) < (i_) * PACE) __builtin_amdgcn_s_sleep(2);    \
    _Pragma("unroll") for (int c_ = 0; c_ < 3; ++c_)                                                              \
        kv[(i_)][c_] = LD128((i_) < NS ? rK : rV, kv_voff + c_ * 128, ((i_) % NS) * TILEB, AUX_NT);               \
  } while (0)
#pragma unroll
  for (int i = 0; i < K0; ++i) ISSUE_KV(i);
  SB();
  const u32 gq_o = tid < 80 ? WS_GQ + (u32)b * 1280u + tid * 16u : OOB;
  const u32 g1_o = lane < 48 ? WS_G1 + (u32)b * 1024u + lane * 16u : OOB;
  const u32 gml_o = WS_GML + (u32)b * 16u;
  const u32 g2_o = lane < 2 * nch ? WS_G2 + (u32)(c0 / 8 + (lane >> 1)) * 2048u + (u32)(h * 2 + rh) * 32u + (u32)(lane & 1) * 16u : OOB;
  if (tid - 256 < L_END - L_CTL && tid >= 256) lds_st(ctl + (tid - 256), 0u);
  SB();
  {
    float a40[40];
#pragma unroll
    for (int c = 0; c < 5; ++c)
#pragma unroll
      for (int k = 0; k < 4; ++k) {
        a40[8 * c + 2 * k] = dot2(__builtin_amdgcn_perm(pb[c][k], pa[c][k], 0x05040100u), xp, 0.f);
        a40[8 * c + 2 * k + 1] = dot2(__builtin_amdgcn_perm(pb[c][k], pa[c][k], 0x07060302u), xp, 0.f);
      }
    reduce_store<40>(a40, lds + L_PQ + w * 320, lane, 1.f);
  }
  __syncthreads();
  const u32 tag = epoch + 1u, tg = tag & TAGM;
  if (w < 2) {
    const int e = tid < 80 ? 4 * tid : 0;
    f32x4 t[NWAVE];
#pragma unroll
    for (int ww = 0; ww < NWAVE; ++ww) t[ww] = *reinterpret_cast<const f32x4*>(lds + L_PQ + ww * 320 + e);
    const f32x4 q4 = ((t[0] + t[1]) + (t[2] + t[3])) + ((t[4] + t[5]) + (t[6] + t[7]));
    const u32x4 pc = {packt(q4[0], tg), packt(q4[1], tg), packt(q4[2], tg), packt(q4[3], tg)};
    ST128(pc, rWS, gq_o, AUX_SC1);
  }
  if (w == 3) {
    float gs = 0.f;
#pragma unroll
    for (int k = 0; k < 4; ++k) { gs = dot2(gx0[k], gw0[k], gs); gs = dot2(gx1[k], gw1[k], gs); }
    gs = sum64(gs);
    if (lane == 0) lds[L_G] = 1.f / (1.f + __expf(-gs));
  }
  if (w == 0) {
    __builtin_amdgcn_s_setprio(3);
    const int piece = lane < 48 ? lane : (lane < 56 ? 48 + 8 * rh + (lane - 48) : 64 + 8 * rh + (lane - 56));
    const u32 off = WS_GQ + (u32)(h * 8) * 1280u + piece * 16u;
    u32x4 gv[8];
    for (u32 spin = 0; spin <= SPIN_CAP; ++spin) {
      asm volatile("" ::: "memory");
#pragma unroll
      for (int i = 0; i < 8; ++i) gv[i] = LD128(rWS, off, i * 1280, AUX_SC1);
      u32 bad = 0;
#pragma unroll
      for (int i = 0; i < 8; ++i) bad |= (gv[i][0] ^ tg) | (gv[i][1] ^ tg) | (gv[i][2] ^ tg) | (gv[i][3] ^ tg);
      if (__all((bad & TAGM) == 0u)) break;
      __builtin_amdgcn_s_sleep(1);
    }
    float v[4], u[4];
#pragma unroll
    for (int k = 0; k < 4; ++k) {
      v[k] = ((unpackt(gv[0][k]) + unpackt(gv[1][k])) + (unpackt(gv[2][k]) + unpackt(gv[3][k]))) +
             ((unpackt(gv[4][k]) + unpackt(gv[5][k])) + (unpackt(gv[6][k]) + unpackt(gv[7][k])));
      u[k] = __shfl_down(v[k], 8);
    }
    if (lane < 48) {
      const u32x2 qq = {f2bf(v[0]) | (f2bf(v[1]) << 16), f2bf(v[2]) | (f2bf(v[3]) << 16)};
      *reinterpret_cast<u32x2*>(lds + L_QB + 2 * lane) = qq;
    } else if (lane < 56) {
#pragma unroll
      for (int k = 0; k < 4; ++k) lds[L_HM + 4 * (lane - 48) + k] = v[k] / (1.f + __expf(-v[k])) * u[k];
    }
    __builtin_amdgcn_s_setprio(0);
  }
  SB();
#pragma unroll
  for (int i = K0; i < LOOK; ++i) ISSUE_KV(i);
  SB();
  __syncthreads();
  u32x4 qv[3];
#pragma unroll
  for (int c = 0; c < 3; ++c) qv[c] = *reinterpret_cast<const u32x4*>(lds + L_QB + (8 * c + j) * 4);
  SB();

  const int hf = lane >> 5, cc = lane & 31;
  const __amdgpu_buffer_rsrc_t rWO = mk_rsrc((const char*)p.wo + (size_t)(h * DH + 96 * rh) * (DM * 2) + c0 * 2,
                                             w < 4 ? (u32)(96 * DM * 2 - c0 * 2) : 0u);
  const __amdgpu_buffer_rsrc_t rW2 =
      mk_rsrc((const char*)p.w2 + (h < NMLP ? (size_t)(h * 64 + 32 * rh) * (DM * 2) + c0 * 2 : 0),
              (h < NMLP && w < 4) ? (u32)(32 * DM * 2 - c0 * 2) : 0u);
  u32x4 wov[12], w2v[4];
  float z[8];
  float sc[NS], acc[24], fw = 0.f;
  u32x4 ml = {0u, 0u, 0u, 0u};
  float mw = 0.f, lg = 0.f;
#pragma unroll
  for (int i = 0; i < 24; ++i) acc[i] = 0.f;
  static_for<LOOK, 2 * NS + LAG>([&](auto ic) __attribute__((always_inline)) {
    constexpr int i = decltype(ic)::value;
    if constexpr (i < 2 * NS) {
      ISSUE_KV(i);
      if constexpr (i == W2_LD) {
        const u32 w2_voff = cc < nch ? (u32)(w * 8 + hf * 4) * (DM * 2) + cc * 16 : OOB;
#pragma unroll
        for (int k = 0; k < 4; ++k) w2v[k] = LD128(rW2, w2_voff, k * (DM * 2), AUX_NT);
      }
      if constexpr (i == W2_USE) {
        const f32x4 hm = *reinterpret_cast<const f32x4*>(lds + L_HM + (w & 3) * 8 + hf * 4);
        const float scb = 1.f - lds[L_G];
#pragma unroll
        for (int k = 0; k < 4; ++k) {
          z[2 * k] = scb * fmaf(hm[3], bflo(w2v[3][k]), fmaf(hm[2], bflo(w2v[2][k]), fmaf(hm[1], bflo(w2v[1][k]), hm[0] * bflo(w2v[0][k]))));
          z[2 * k + 1] = scb * fmaf(hm[3], bfhi(w2v[3][k]), fmaf(hm[2], bfhi(w2v[2][k]), fmaf(hm[1], bfhi(w2v[1][k]), hm[0] * bfhi(w2v[0][k]))));
        }
#pragma unroll
        for (int k = 0; k < 8; ++k) asm volatile("" : "+v"(z[k]));
      }
      if constexpr (i == ML_LD) ml = LD128(rWS, WS_GML + (u32)(h * 8 + j) * 16u, 0, AUX_SC1);
      if constexpr (i == ML_USE) {
        bool mok = __all(hast(ml, tg));
        SB();
        if (__builtin_expect(!mok, 0)) {
          for (u32 spin = 0; !mok && spin <= SPIN_CAP; ++spin) {
            asm volatile("" ::: "memory");
            ml = LD128(rWS, WS_GML + (u32)(h * 8 + j) * 16u, 0, AUX_SC1);
            mok = __all(hast(ml, tg));
          }
        }
        const float ms = unsplit(ml[0], ml[1]);
        float M = fmaxf(ms, dpp<XOR1>(ms)); M = fmaxf(M, dpp<XOR2>(M)); M = fmaxf(M, dpp<XOR7>(M));
        float L = __builtin_amdgcn_exp2f((ms - M) * C2) * unsplit(ml[2], ml[3]);
        L += dpp<XOR1>(L); L += dpp<XOR2>(L); L += dpp<XOR7>(L);
        fw = lds[L_G] * __builtin_amdgcn_exp2f((mw - M) * C2) * __builtin_amdgcn_rcpf(L);
        asm volatile("" : "+v"(fw));
      }
    } else if constexpr (i == 2 * NS + LAG - 2) {
      if (w < 4)
        while ((int)((u32)__builtin_amdgcn_s_memrealtime() - t_pace) < 2 * NS * PACE + WDLY - w * PACE / 8) __builtin_amdgcn_s_sleep(1);
      const u32 wo_voff = cc < nch ? (u32)(w * 24 + hf * 12) * (DM * 2) + cc * 16 : OOB;
#pragma unroll
      for (int k = 0; k < 12; ++k) wov[k] = LD128(rWO, wo_voff, k * (DM * 2), AUX_NT);
    }
    SB();
    static_for<cons_end(i - 1), cons_end(i)>([&](auto tc) __attribute__((always_inline)) {
      constexpr int t = decltype(tc)::value;
      if constexpr (t < NS) {
        float d0 = 0.f, d1 = 0.f, d2 = 0.f;
#pragma unroll
        for (int k = 0; k < 4; ++k) {
          d0 = dot2(kv[t][0][k], qv[0][k], d0);
          d1 = dot2(kv[t][1][k], qv[1][k], d1);
          d2 = dot2(kv[t][2][k], qv[2][k], d2);
        }
        float d = (d0 + d1) + d2;
        d += dpp<XOR1>(d); d += dpp<XOR2>(d); d += dpp<XOR7>(d);
        asm volatile("" : "+v"(d));
        sc[t] = d;
        if constexpr (t == NS - 1) {
          mw = sc[0];
#pragma unroll
          for (int u = 1; u < NS; ++u) mw = fmaxf(mw, sc[u]);
          mw = fmaxf(mw, dpp<XOR8>(mw)); mw = max16(mw); mw = max32(mw);
          const float mc = mw * C2;
#pragma unroll
          for (int u = 0; u < NS; ++u) { sc[u] = __builtin_amdgcn_exp2f(fmaf(sc[u], C2, -mc)); lg += sc[u]; }
          lg += dpp<XOR8>(lg); lg = rs16(lg, lg); lg = rs32(lg, lg);
          if (lane == 0) { lds[L_MW + w] = mw; lds[L_LW + w] = lg; }
          LDS_FENCE();
          u32 ka = 0;
          if (lane == 0) ka = __hip_atomic_fetch_add(ctl + C_KCNT, 1u, __ATOMIC_RELAXED, __HIP_MEMORY_SCOPE_WORKGROUP);
          ka = __builtin_amdgcn_readfirstlane(ka);
          asm volatile("" ::: "memory");
          if (ka == NWAVE - 1) {
            const float mj = lds[L_MW + j], lj = lds[L_LW + j];
            float M = fmaxf(mj, dpp<XOR1>(mj)); M = fmaxf(M, dpp<XOR2>(M)); M = fmaxf(M, dpp<XOR7>(M));
            float L = __builtin_amdgcn_exp2f((mj - M) * C2) * lj;
            L += dpp<XOR1>(L); L += dpp<XOR2>(L); L += dpp<XOR7>(L);
            const u32x4 pm = {(f2u(M) & ~TAGM) | tg, ((f2u(M) & TAGM) << 12) | tg, (f2u(L) & ~TAGM) | tg, ((f2u(L) & TAGM) << 12) | tg};
            ST128(pm, rWS, lane == 0 ? gml_o : OOB, AUX_SC1);
          }
        }
      } else {
        const float pt = sc[t - NS];
#pragma unroll
        for (int c = 0; c < 3; ++c)
#pragma unroll
          for (int k = 0; k < 4; ++k) {
            const u32 vv = kv[t][c][k];
            acc[8 * c + 2 * k] = fmaf(pt, bflo(vv), acc[8 * c + 2 * k]);
            acc[8 * c + 2 * k + 1] = fmaf(pt, bfhi(vv), acc[8 * c + 2 * k + 1]);
          }
        PIN24(acc);
      }
      SB();
    });
  });
  reduce_store<24>(acc, lds + L_PART + w * DH, lane, fw);
  LDS_FENCE();
  u32 arr = 0;
  if (lane == 0) arr = __hip_atomic_fetch_add(ctl + C_PCNT, 1u, __ATOMIC_RELAXED, __HIP_MEMORY_SCOPE_WORKGROUP);
  arr = __builtin_amdgcn_readfirstlane(arr);
  asm volatile("" ::: "memory");
  if (arr == NWAVE - 1) {
    const int d = lane < 48 ? 4 * lane : 0;
    f32x4 pt[NWAVE];
#pragma unroll
    for (int ww = 0; ww < NWAVE; ++ww) pt[ww] = *reinterpret_cast<const f32x4*>(lds + L_PART + ww * DH + d);
    const f32x4 o = ((pt[0] + pt[1]) + (pt[2] + pt[3])) + ((pt[4] + pt[5]) + (pt[6] + pt[7]));
    const u32x4 pv = {packt(o[0], tg), packt(o[1], tg), packt(o[2], tg), packt(o[3], tg)};
    ST128(pv, rWS, g1_o, AUX_SC1);
  }

  if (w >= 4) {
    if (b < 80 && w == 4) {
      const u32 off = WS_G2 + (u32)b * 2048u + (u32)cc * 32u + (u32)hf * 16u;
      u32x4 va = {0u, 0u, 0u, 0u}, vb = va;
      bool ok0 = false, ok1 = false;
      for (u32 spin = 0; spin <= SPIN_CAP; ++spin) {
        asm volatile("" ::: "memory");
        const u32x4 na = LD128(rWS, ok0 ? OOB : off, 0, AUX_SC1), nb = LD128(rWS, ok1 ? OOB : off + 1024u, 0, AUX_SC1);
        if (!ok0) { va = na; ok0 = hast(na, tg); }
        if (!ok1) { vb = nb; ok1 = hast(nb, tg); }
        if (__all(ok0 && ok1)) break;
      }
      float v[4];
#pragma unroll
      for (int k = 0; k < 4; ++k) v[k] = unpackt(va[k]) + unpackt(vb[k]);
      const float x0 = rs16(v[0], v[1]), x1 = rs16(v[2], v[3]);
      const float t0 = x0 + dpp<XOR8>(x0), t1 = x1 + dpp<XOR8>(x1);
      float y = (lane & 8) ? t1 : t0;
      y += dpp<XOR1>(y); y += dpp<XOR2>(y); y += dpp<XOR7>(y);
      const __amdgpu_buffer_rsrc_t rOUT = mk_rsrc(p.out, DM * 2);
      const u32 col = 4 * hf + 2 * ((lane >> 3) & 1) + ((lane >> 4) & 1);
      __builtin_amdgcn_raw_buffer_store_b16((unsigned short)f2bf(y), rOUT, j == 0 ? (u32)b * 16u + col * 2u : OOB, 0, AUX_SC1);
      __builtin_amdgcn_raw_buffer_store_b32(tag, rWS, (b == 0 && lane == 0) ? WS_EPOCH : OOB, 0, AUX_SC1);
    }
    return;
  }

  u32 wp[6][8];
#pragma unroll
  for (int i = 0; i < 6; ++i)
#pragma unroll
    for (int k = 0; k < 4; ++k) {
      wp[i][2 * k] = __builtin_amdgcn_perm(wov[2 * i + 1][k], wov[2 * i][k], 0x05040100u);
      wp[i][2 * k + 1] = __builtin_amdgcn_perm(wov[2 * i + 1][k], wov[2 * i][k], 0x07060302u);
    }
#pragma unroll
  for (int i = 0; i < 6; ++i)
#pragma unroll
    for (int k = 0; k < 8; ++k) asm volatile("" : "+v"(wp[i][k]));
  {
    const u32 off = WS_G1 + (u32)(h * 8 + j) * 1024u + (u32)(24 * rh + 6 * w + r) * 16u;
    u32x4 va = {0u, 0u, 0u, 0u};
    bool ok = r >= 6;
    for (u32 spin = 0; spin <= SPIN_CAP; ++spin) {
      asm volatile("" ::: "memory");
      const u32x4 na = LD128(rWS, ok ? OOB : off, 0, AUX_SC1);
      if (!ok) { va = na; ok = hast(na, tg); }
      if (__all(ok)) break;
    }
    float c[4];
#pragma unroll
    for (int k = 0; k < 4; ++k) {
      float t = unpackt(va[k]);
      t += dpp<XOR1>(t); t += dpp<XOR2>(t); t += dpp<XOR7>(t);
      c[k] = t;
    }
    const u32 pk0 = f2bf(c[0]) | (f2bf(c[1]) << 16), pk1 = f2bf(c[2]) | (f2bf(c[3]) << 16);
    u32 cp[6];
#pragma unroll
    for (int i = 0; i < 6; ++i)
      cp[i] = __builtin_amdgcn_ds_bpermute((3 * hf + (i >> 1)) * 32, (i & 1) ? pk1 : pk0);
#pragma unroll
    for (int i = 0; i < 6; ++i)
#pragma unroll
      for (int k = 0; k < 8; ++k) z[k] = dot2(wp[i][k], cp[i], z[k]);
    f32x4 z4;
#pragma unroll
    for (int k = 0; k < 4; ++k) z4[k] = rs32(z[k], z[k + 4]);
    if (cc < 24) *reinterpret_cast<f32x4*>(lds + L_Z + w * 192 + cc * 8 + hf * 4) = z4;
    LDS_FENCE();
  }
  u32 zar = 0;
  if (lane == 0) zar = __hip_atomic_fetch_add(ctl + C_ZCNT, 1u, __ATOMIC_RELAXED, __HIP_MEMORY_SCOPE_WORKGROUP);
  zar = __builtin_amdgcn_readfirstlane(zar);
  asm volatile("" ::: "memory");
  if (zar == 3u) {
    const int c = lane < 48 ? 4 * lane : 0;
    f32x4 t = *reinterpret_cast<const f32x4*>(lds + L_Z + c);
#pragma unroll
    for (int ww = 1; ww < 4; ++ww) t += *reinterpret_cast<const f32x4*>(lds + L_Z + ww * 192 + c);
    const u32x4 pv = {packt(t[0], tg), packt(t[1], tg), packt(t[2], tg), packt(t[3], tg)};
    ST128(pv, rWS, g2_o, AUX_SC1);
  }
}

}

extern "C" void kernel_launch(void* const* d_in, const int* in_sizes, int n_in, void* d_out, int out_size, void* d_ws,
                              size_t ws_size, hipStream_t stream) {
  (void)in_sizes; (void)n_in; (void)out_size; (void)ws_size;
  Params p;
  p.x = d_in[0]; p.k = d_in[1]; p.v = d_in[2]; p.wq = d_in[3]; p.wo = d_in[4];
  p.w1 = d_in[6]; p.w3 = d_in[7]; p.w2 = d_in[8]; p.gw = d_in[9];
  p.out = d_out; p.ws = d_ws;
  hipLaunchKernelGGL(fused_decode_kernel, dim3(NWG), dim3(NTHR), 0, stream, p);
}
```
